```python
import torch

def _softmax(x, axis=-1):
    return torch.softmax(x, dim=axis)
def _gelu(x):
    return torch.nn.functional.gelu(x, approximate="tanh")
def _silu(x):
    return torch.nn.functional.silu(x)
def _sigmoid(x):
    return torch.sigmoid(x)
def _layernorm(x, axis=-1, eps=1e-5):
    m = x.mean(dim=axis, keepdim=True)
    v = x.var(dim=axis, keepdim=True, unbiased=False)
    return (x - m) / torch.sqrt(v + eps)
def _scatter_add(tgt, idx, src, axis=0):
    return tgt.clone().index_add_(axis, idx, src)
def _segment_sum(src, seg, n):
    out = torch.zeros((n,) + src.shape[1:], dtype=src.dtype)
    return out.index_add_(0, seg, src)
def _pad(x, pw):
    flat = tuple(v for pair in reversed(pw) for v in pair)
    return torch.nn.functional.pad(x, flat)

kernel_name = 'standalone__linear_kernel_rfa__decode'

def setup_inputs():

    _w = (lambda a: (lambda t: t.to(torch.bfloat16) if t.is_floating_point() else t)(torch.as_tensor(a)))
    B, H, Sq, Sk, D, M = 2, 16, 1, 16384, 256, 16
    return {
        "q": _w((lambda *s: torch.randn(*s, dtype=torch.bfloat16) * 0.4)(B, H, Sq, D)),
        "k": _w((lambda *s: torch.randn(*s, dtype=torch.bfloat16) * 1.6)(B, H, Sk, D)),
        "v": _w((lambda *s: torch.randn(*s, dtype=torch.bfloat16) * 1.6)(B, H, Sk, D)),
        "omega": _w((lambda *s: torch.randn(*s, dtype=torch.bfloat16) * 1.6)(M, D)),
    }

def reference(q, k, v, omega):
    d = q.shape[-1]
    m = omega.shape[0]
    scale = float(d) ** -0.25
    qs = q * scale
    ks = k * scale
    q_proj = torch.einsum("bhsd,md->bhsm", qs, omega)
    k_proj = torch.einsum("bhsd,md->bhsm", ks, omega)
    q_sq = 0.5 * torch.einsum("bhsd,bhsd->bhs", qs, qs)[..., None]
    k_sq = 0.5 * torch.einsum("bhsd,bhsd->bhs", ks, ks)[..., None]
    inv_sqrt_m = float(m) ** -0.5
    phi_q = torch.exp(q_proj - q_sq) * inv_sqrt_m + 1e-6
    phi_k = torch.exp(k_proj - k_sq) * inv_sqrt_m + 1e-6
    kv = torch.einsum("bhsm,bhsd->bhmd", phi_k, v)
    z  = torch.einsum("bhsm->bhm", phi_k)
    num = torch.einsum("bhqm,bhmd->bhqd", phi_q, kv)
    den = torch.einsum("bhqm,bhm->bhq", phi_q, z)[..., None] + 1e-6
    return num / den
```

```cpp
#include <hip/hip_runtime.h>
#include <hip/hip_bf16.h>

typedef unsigned u32x4 __attribute__((ext_vector_type(4)));
typedef float f32x4 __attribute__((ext_vector_type(4)));
typedef float f32x2 __attribute__((ext_vector_type(2)));
typedef __bf16 bf16x8 __attribute__((ext_vector_type(8)));
typedef __bf16 bf16x2 __attribute__((ext_vector_type(2)));
typedef unsigned long long u64;
typedef __attribute__((address_space(1))) u64 gu64;
typedef __attribute__((address_space(1))) __bf16 gbf16;
typedef __attribute__((address_space(3))) unsigned lu32;
typedef __attribute__((address_space(3))) u64 lu64;
typedef __attribute__((address_space(4))) const unsigned cu32;
typedef __amdgpu_buffer_rsrc_t rsrc_t;

#define LLD(p) __hip_atomic_load(p, __ATOMIC_RELAXED, __HIP_MEMORY_SCOPE_WORKGROUP)
#define LST(p, x) __hip_atomic_store(p, x, __ATOMIC_RELAXED, __HIP_MEMORY_SCOPE_WORKGROUP)
#define GLD(p) __hip_atomic_load(p, __ATOMIC_RELAXED, __HIP_MEMORY_SCOPE_AGENT)
#define GST(p, x) __hip_atomic_store(p, x, __ATOMIC_RELAXED, __HIP_MEMORY_SCOPE_AGENT)
#define NOUNROLL _Pragma("clang loop unroll(disable)")
#define FENCE_SCHED() __builtin_amdgcn_sched_barrier(0)
#define PIN(x) asm volatile("" : "+v"(x))
#define BARRIER()                                       \
  do {                                                  \
    asm volatile("s_waitcnt lgkmcnt(0)" ::: "memory");  \
    __builtin_amdgcn_s_barrier();                       \
    FENCE_SCHED();                                      \
    asm volatile("" ::: "memory");                      \
  } while (0)

constexpr int BH = 32, SK = 16384, NMEM = 8, NWAVE = 4;
constexpr int NTHREADS = (NWAVE + 1) * 64;
constexpr int NCH = 32;
constexpr unsigned HEAD_BYTES = SK * 512;
constexpr int SPIN = 1 << 20;
constexpr unsigned CODE_BYTES = 5896;

constexpr int RING = 6;
constexpr int LEAD = 35 * 16;
constexpr int CP0 = 1, CPS = 12, NRD = 5;
constexpr int RDD = 2;
constexpr int MARGIN = 8 * 16, SLOWX = 20 * 16, NOMINAL = 1704;

constexpr int WS_E = 0, WS_T = 64, WS_D = WS_T + 8 * 64, WS_G = WS_D + 256;
constexpr int R4 = 33, T4 = 16 * R4, TILES = 22 * R4;
constexpr int L_O = 0, L_K = 17 * R4, L_W = L_K + NWAVE * TILES, L_T = L_W + NWAVE * (NCH + 1) * 4, L_C = L_T + 64, L_END = L_C + 2;

__device__ __forceinline__ float asf(unsigned x) { return __builtin_bit_cast(float, x); }
__device__ __forceinline__ unsigned asu(float x) { return __builtin_bit_cast(unsigned, x); }
__device__ __forceinline__ unsigned hi32(u64 x) { return (unsigned)(x >> 32); }
__device__ __forceinline__ unsigned clock10ns() { return (unsigned)__builtin_amdgcn_s_memrealtime(); }
__device__ __forceinline__ float rbf(float x, float zero) {
  return __builtin_bit_cast(float, __builtin_convertvector(f32x2{zero, x}, bf16x2));
}
template <int CTRL> __device__ __forceinline__ int dppi(int x) { return __builtin_amdgcn_mov_dpp(x, CTRL, 0xf, 0xf, true); }
template <int CTRL> __device__ __forceinline__ float dpp(float x) { return asf(dppi<CTRL>(asu(x))); }
constexpr int XOR1 = 0xb1, XOR2 = 0x4e, XOR7 = 0x141, ROR8 = 0x128;
__device__ __forceinline__ f32x4 mfma(u32x4 a, u32x4 b, f32x4 c) {
  return __builtin_amdgcn_mfma_f32_16x16x32_bf16(__builtin_bit_cast(bf16x8, a), __builtin_bit_cast(bf16x8, b), c, 0, 0, 0);
}
__device__ __forceinline__ f32x4 colsum(float x) {
  return __builtin_amdgcn_mfma_f32_16x16x4f32(1.0f, x, f32x4{0.f, 0.f, 0.f, 0.f}, 0, 0, 0);
}
__device__ __forceinline__ float fold32(float a, float b) {
  const auto s = __builtin_amdgcn_permlane32_swap(asu(a), asu(b), false, false);
  return asf(s[0]) + asf(s[1]);
}
__device__ __forceinline__ rsrc_t mkrs(const void* p, unsigned bytes) {
  return __builtin_amdgcn_make_buffer_rsrc((void*)p, 0, bytes, 0x00020000);
}
__device__ __forceinline__ float phi(float dot, float kr, float z0) {
  const float x4 = rbf(fmaf(kr, -0.125f, rbf(dot, z0)), z0);
  return rbf(fmaf(rbf(__builtin_amdgcn_exp2f(x4 * (0.25f * 1.44269504088896341f)), z0), 0.25f, 1e-6f), z0);
}

__global__ void __launch_bounds__(NTHREADS, 2)
rfa_decode(const unsigned char* __restrict__ q, const unsigned char* __restrict__ k, const unsigned char* __restrict__ v,
           const unsigned char* __restrict__ omega, __hip_bfloat16* __restrict__ out, u64* __restrict__ ws_) {
  const unsigned pf = __builtin_amdgcn_raw_buffer_load_b32(
      mkrs((const void*)(__builtin_amdgcn_s_getpc() & ~127ull), CODE_BYTES), (threadIdx.x ^ 256) * 128, 0, 0);
  FENCE_SCHED();
  __shared__ u32x4 l4[L_END];
  float* lds = (float*)l4;
  asm volatile("" : "+s"(q), "+s"(k), "+s"(v), "+s"(omega), "+s"(out), "+s"(ws_));
  float z0 = 0.f;
  asm volatile("" : "+s"(z0));
  const int tid = threadIdx.x, lane = tid & 63;
  const int wave = __builtin_amdgcn_readfirstlane(tid >> 6);
  const unsigned bid = blockIdx.x, bh = bid / NMEM, mem = bid % NMEM;
  gu64* ws = (gu64*)ws_;
  const unsigned char* kh = k + (size_t)bh * HEAD_BYTES;
  const unsigned char* vh = v + (size_t)bh * HEAD_BYTES;
  lu32* lc = (lu32*)(l4 + L_C);
  lu32* lt = (lu32*)(l4 + L_T);

  if (wave == NWAVE) {
    const unsigned now0 = clock10ns();
    l4[L_T + lane] = u32x4{0, 0, 0, 0};
    LST((lu64*)lc, 0ull);
    BARRIER();
    const u64 e0 = GLD(ws + WS_E);
    const u64 pre0 = GLD(ws + WS_G + (bh * NMEM + (lane >> 1 & 7)) * 256 + mem * 32 + (lane & 1) * 16);
    const unsigned pre1 = __builtin_amdgcn_raw_buffer_load_b32(mkrs(ws_, bid < 8 ? WS_G * 8 : 0), lane * 128, 0, 16);
    FENCE_SCHED();
    const unsigned tag = hi32(e0 * 0x9E3779B97F4A7C15ull + 0x632BE59BD9B4E019ull);
    const u64 th = (u64)tag << 32;
    LST(lc + 2, tag);
    unsigned touch = 0;
#pragma unroll
    for (int i = 0; i < 4; ++i)
      touch ^= *(cu32*)(vh + i * 2101504 + mem * 8192) ^ *(cu32*)(kh + i * 2101504 + mem * 8192 + 1048576);
    asm volatile("" ::"v"(touch), "v"(pre0), "v"(pre1), "v"(pf));

    int mprev = 0, cprev = -1, sprev = 0, nrd = NRD;
    bool den_out = false;
    asm volatile("" : "+s"(nrd));
    NOUNROLL for (int rd = 0; rd < nrd; ++rd) {
      const int cp = CP0 + rd * CPS;
      unsigned t;
      NOUNROLL do { __builtin_amdgcn_s_sleep(8); t = LLD(lt + cp * 4 + (lane & 3)); } while (!__all(t != 0));
      asm volatile("" ::: "memory");
      if (cp > NCH && !den_out) {
        const f32x4 d4 = __builtin_bit_cast(f32x4, l4[L_C + 1]);
        if (lane == 0) GST(ws + WS_D + bid, th | asu((d4[0] + d4[1]) + (d4[2] + d4[3])));
        den_out = true;
      }
      gu64* gt = ws + WS_T + rd * 64;
      if ((bh & 15) == 8) {
        t -= now0;
        t += dppi<XOR1>(t);
        t += dppi<XOR2>(t);
        if (lane == 0) GST(gt + (bh >> 4) * NMEM + mem, th | ((t >> 2) + now0));
      }
      u64 x;
      bool got = false;
      NOUNROLL for (int cw = cp + RDD; !got && cw < cp + RDD + 4; ++cw) {
        NOUNROLL do { __builtin_amdgcn_s_sleep(8); } while (LLD(lt + cw * 4) == 0);
        x = GLD(gt + (lane & 15));
        got = __all(hi32(x) == tag);
      }
      if (got) {
        int d = (int)((unsigned)x - now0);
        d += dppi<ROR8>(d);
        int y = max(d, dppi<XOR1>(d));
        y = max(y, dppi<XOR2>(y));
        y = max(y, dppi<XOR7>(y));
        d += dppi<XOR1>(d);
        d += dppi<XOR2>(d);
        d += dppi<XOR7>(d);
        const int mean = __builtin_amdgcn_readfirstlane(d), slowest = __builtin_amdgcn_readfirstlane(y) * 8;
        const int sm = cprev < 0 ? NOMINAL + NOMINAL / 64 : (int)((float)(mean - mprev) * 0.995f / (float)(cp - cprev));
        const int sl = cprev < 0 ? NOMINAL : max(NOMINAL, min(sm, sprev));
        LST((lu64*)lc, (u64)(unsigned)sl << 32 | (now0 * 16 + (unsigned)(max(mean + MARGIN, slowest - SLOWX) - sl * cp)));
        mprev = mean; cprev = cp; sprev = sm;
      }
    }

    float n, dn;
    bool ok;
    NOUNROLL for (int spin = 0; spin < SPIN; ++spin) {
      const u64 xd = GLD(ws + WS_D + bh * NMEM + (lane & 7));
      dn = asf((unsigned)xd);
      ok = __all(hi32(xd) == tag);
      if (ok) break;
      __builtin_amdgcn_s_sleep(8);
    }
    dn += dpp<XOR1>(dn);
    dn += dpp<XOR2>(dn);
    dn += dpp<XOR7>(dn);
    dn = ok ? 1.0f / rbf(rbf(dn, z0) + 1e-6f, z0) : __builtin_nanf("");
    if (bid == 0) {
      NOUNROLL for (int spin = 0; spin < SPIN; ++spin) {
        gu64* gd = ws + WS_D + lane;
        const u64 x0 = GLD(gd), x1 = GLD(gd + 64), x2 = GLD(gd + 128), x3 = GLD(gd + 192);
        if (__all(hi32(x0) == tag && hi32(x1) == tag && hi32(x2) == tag && hi32(x3) == tag)) break;
        __builtin_amdgcn_s_sleep(8);
      }
      if (lane == 0) GST(ws + WS_E, e0 + 1);
    }

    gu64* gm = ws + WS_G + (bh * NMEM + (lane >> 5) * 4 + 2) * 256 + mem * 32 + (lane & 31);
    gbf16* po = (gbf16*)out + bid * 32 + (lane & 31);
    asm volatile("" : "+v"(gm), "+v"(dn), "+v"(po));
    BARRIER();
    NOUNROLL for (int spin = 0; spin < SPIN; ++spin) {
      const u64 x0 = GLD(gm - 512), x1 = GLD(gm - 256), x2 = GLD(gm), x3 = GLD(gm + 256);
      n = (asf((unsigned)x0) + asf((unsigned)x1)) + (asf((unsigned)x2) + asf((unsigned)x3));
      ok = __builtin_amdgcn_ballot_w64(((hi32(x0) ^ tag) | (hi32(x1) ^ tag) | (hi32(x2) ^ tag) | (hi32(x3) ^ tag)) != 0) == 0;
      if (ok) break;
    }
    n = fold32(n, n);
    if (!ok) dn = __builtin_nanf("");
    *po = (__bf16)(rbf(n, z0) * dn);
    return;
  }

  const int g = lane >> 4, c = lane & 15;
  const int xj = (wave & 1) * 6;
  int vo[4];
#pragma unroll
  for (int i = 0; i < 4; ++i) vo[i] = lane * 16 + ((i ^ xj) & 3) * 1024;
  const unsigned so0 = mem * 32768 + wave * 4096 + (xj >> 2) * 16384, so1 = so0 ^ 16384;
  u32x4 r[8], om[8];
#define LOAD(rs, n, j) __builtin_amdgcn_raw_buffer_load_b128(rs, vo[(j) & 3], (n) * 262144 + ((j) < 4 ? so0 : so1), 2)
  {
    const rsrc_t rk = mkrs(kh, HEAD_BYTES), ro = mkrs(omega, 8192);
    const u32x4 o0 = __builtin_amdgcn_raw_buffer_load_b128(ro, tid * 16, 0, 0);
    const u32x4 o1 = __builtin_amdgcn_raw_buffer_load_b128(ro, tid * 16, 4096, 0);
    const u32x4 oq = __builtin_amdgcn_raw_buffer_load_b128(mkrs(q + bh * 512, 512), (tid & 31) * 16, 0, 0);
    FENCE_SCHED();
#pragma unroll
    for (int j = 0; j < RING; ++j) { r[j] = LOAD(rk, 0, j); FENCE_SCHED(); }
    l4[L_O + (tid >> 5) * R4 + (tid & 31)] = o0;
    l4[L_O + ((tid >> 5) + 8) * R4 + (tid & 31)] = o1;
    l4[L_O + 16 * R4 + (tid & 31)] = oq;
    asm volatile("" ::"v"(pf));
    BARRIER();
  }

  int lag = -1;
  unsigned now;
  u64 ln;
#define PACE_READ() now = clock10ns(), ln = LLD((lu64*)lc)
#define PACE_EVAL(n)                  \
  LST(lt + (n) * 4 + wave, now | 1);  \
  lag = __builtin_amdgcn_readfirstlane(hi32(ln) == 0 ? -1 : (int)(now * 16 - (unsigned)ln - hi32(ln) * (n)))
#define ARRIVE(j, rc, nc, rn, nn)                                                                       \
  asm volatile("" : "+v"(r[j])::"memory");                                                              \
  asm volatile("s_cmp_ge_i32 %0, 0\n\ts_cbranch_scc1 1f\n\ts_waitcnt vmcnt(%1)\n\t"                     \
               "s_cmp_gt_i32 %0, %2\n\ts_cbranch_scc1 1f\n\ts_waitcnt vmcnt(%3)\n1:"                    \
               ::"s"(lag), "i"(RING - 2), "i"(-LEAD), "i"(RING - 3) : "scc", "memory");                 \
  if ((j) + RING < 8) r[((j) + RING) & 7] = LOAD(rc, nc, ((j) + RING) & 7);                             \
  else r[((j) + RING) & 7] = LOAD(rn, nn, ((j) + RING) & 7);                                            \
  FENCE_SCHED()

  const f32x4 z = {0.f, 0.f, 0.f, 0.f};
  const bool odd = c & 1, hi2 = c & 2, diag = (c >> 2) == g;
  float den = 0.f, kr, wp;
  f32x4 phq = z, dot, gr, s4;
  float* wl = lds + (L_W + wave * (NCH + 1) * 4) * 4 + (c & 1) * 8 + (c >> 1);
  const int tw0 = L_K + wave * TILES + (lane >> 5) * R4 + (lane & 31), tr0 = L_K + wave * TILES + c * R4 + g;
  int tw = tw0, tr = L_O + 16 * R4 + g;
  u32x4 fa, fb;
#define FRAGS(s) fa = l4[tr + (s) * 4], fb = l4[tr + (s) * 4 + 4]
#define CHAIN(e)                                         \
  {                                                      \
    const float ph = phi(dot[e], kr, z0);                \
    wp = (e) == 0 ? ph * phq[0] : fmaf(ph, phq[e], wp);  \
    phq[e] = first ? ph : phq[e];                        \
  }
#pragma unroll
  for (int s = 0; s < 8; ++s) om[s] = l4[L_O + c * R4 + s * 4 + g];
  FRAGS(0);
  int nch = NCH;
  asm volatile("" : "+s"(nch));
  int n = 0;
  NOUNROLL do {
    int nn = n;
    asm volatile("" : "+s"(nn));
    const bool first = nn == 0;
    const rsrc_t rc = mkrs(n >= nch ? vh : kh, HEAD_BYTES), rn = mkrs(n + 1 >= nch ? vh : kh, HEAD_BYTES);
    const int n0 = n & (NCH - 1), n1 = (n + 1) & (NCH - 1);
#pragma unroll
    for (int j = 0; j < 8; ++j) {
      ARRIVE(j, rc, n0, rn, n1);
      l4[(j >= 3 ? tw0 : tw) + j * 2 * R4] = r[j];
      if (j < 4) {
        dot = mfma(om[2 * j], fa, j == 0 ? z : dot);
        gr = mfma(fa, fa, j == 0 ? z : gr);
        dot = mfma(om[2 * j + 1], fb, dot);
        gr = mfma(fb, fb, gr);
        PIN(dot); PIN(gr);
      }
      if (j < 3) FRAGS(2 * j + 2);
      if (j == 0) PACE_READ();
      if (j == 1) { PACE_EVAL(n); }
      if (j == 4) {
        const float g0 = gr[0], g1 = gr[1], g2 = gr[2], g3 = gr[3];
        const float ge = hi2 ? g2 : g0, go = hi2 ? g3 : g1, gd = odd ? go : ge;
        s4 = colsum(diag ? gd : 0.f);
        PIN(s4);
      }
      if (j == 5) { kr = rbf(s4[0], z0); CHAIN(0) CHAIN(1) PIN(wp); PIN(phq); }
      if (j == 6) { CHAIN(2) CHAIN(3) s4 = colsum(wp); PIN(s4); PIN(phq); }
      if (j == 7) {
        den += s4[0];
        wl[n * 16] = s4[0];
        PIN(den);
        tr = tr0 + (n & 1) * (c >= 6 ? 0 : T4);
        tw = tw0 + (n1 & 1) * T4;
        FRAGS(0);
      }
      FENCE_SCHED();
    }
  } while (++n <= nch);
  den += dpp<XOR1>(den);
  den += dpp<XOR2>(den);
  den += dpp<XOR7>(den);
  den += dpp<ROR8>(den);
  LST(lc + 4 + wave, asu(den));

  float acc[8];
#pragma unroll
  for (int e = 0; e < 8; ++e) acc[e] = 0.f;
#define FMA8(w, x)                                                   \
  _Pragma("unroll") for (int e = 0; e < 4; ++e) {                    \
    const unsigned y = x[e];                                         \
    acc[2 * e] = fmaf(w, asf(y << 16), acc[2 * e]);                  \
    acc[2 * e + 1] = fmaf(w, asf(y & 0xffff0000u), acc[2 * e + 1]);  \
  }                                                                  \
  asm volatile("" : "+v"(acc[0]), "+v"(acc[1]), "+v"(acc[2]), "+v"(acc[3]), "+v"(acc[4]), "+v"(acc[5]), "+v"(acc[6]), "+v"(acc[7]))
  const rsrc_t rv = mkrs(vh, HEAD_BYTES);
  const int wr = L_W + wave * (NCH + 1) * 4 + 4 + (lane >> 5) * 2;
  f32x4 wa = __builtin_bit_cast(f32x4, l4[wr + 4]), wb;
  gu64* gp = ws + WS_G + bid * 256 + tid;
  int lp = wave * 64 + (lane & 31) * 2 + (lane >> 5);
  asm volatile("" : "+v"(gp), "+v"(lp));
  n = 1;
  NOUNROLL do {
    const rsrc_t rn = mkrs(vh, n + 1 < nch ? HEAD_BYTES : 0);
    float w0;
#pragma unroll
    for (int j = 0; j < 8; ++j) {
      ARRIVE(j, rv, n, rn, n + 1);
      if (j == 0) { PACE_READ(); wb = __builtin_bit_cast(f32x4, l4[wr + n * 4 + 1]); }
      if (j == 1) { PACE_EVAL(n + NCH); lag = n + 1 < nch ? lag : 0; }
      if (j == 2) { fa = l4[tw0 + ((n - 1) & 7) * 2 * R4]; w0 = lds[wr * 4 + ((n - 1) & 7)]; }
      const float w = j < 4 ? wa[j & 3] : wb[j & 3];
      FMA8(w, r[j]);
      if (j == 3 && n <= 8) { FMA8(w0, fa); }
      if (j == 4) wa = __builtin_bit_cast(f32x4, l4[wr + min(n + 1, NCH - 1) * 4]);
      FENCE_SCHED();
    }
  } while (++n < nch);

  l4[lp] = u32x4{asu(fold32(acc[0], acc[4])), asu(fold32(acc[1], acc[5])), asu(fold32(acc[2], acc[6])), asu(fold32(acc[3], acc[7]))};
  BARRIER();
  const unsigned tag = LLD(lc + 2);
  const float num = (lds[tid] + lds[512 + tid]) + (lds[256 + tid] + lds[768 + tid]);
  GST(gp, (u64)tag << 32 | asu(num));
}

extern "C" void kernel_launch(void* const* d_in, const int* in_sizes, int n_in, void* d_out, int out_size, void* d_ws,
                              size_t ws_size, hipStream_t stream) {
  rfa_decode<<<BH * NMEM, NTHREADS, 0, stream>>>((const unsigned char*)d_in[0], (const unsigned char*)d_in[1],
                                                 (const unsigned char*)d_in[2], (const unsigned char*)d_in[3],
                                                 (__hip_bfloat16*)d_out, (u64*)d_ws);
}
```

```cpp
#include <hip/hip_runtime.h>
#include <hip/hip_bf16.h>

typedef unsigned u32x4 __attribute__((ext_vector_type(4)));
typedef float f32x4 __attribute__((ext_vector_type(4)));
typedef float f32x2 __attribute__((ext_vector_type(2)));
typedef __bf16 bf16x8 __attribute__((ext_vector_type(8)));
typedef __bf16 bf16x2 __attribute__((ext_vector_type(2)));
typedef unsigned long long u64;
typedef __attribute__((address_space(1))) u64 gu64;
typedef __attribute__((address_space(1))) __bf16 gbf16;
typedef __attribute__((address_space(3))) unsigned lu32;
typedef __attribute__((address_space(3))) u64 lu64;
typedef __attribute__((address_space(4))) const unsigned cu32;
typedef __amdgpu_buffer_rsrc_t rsrc_t;

#define LLD(p) __hip_atomic_load(p, __ATOMIC_RELAXED, __HIP_MEMORY_SCOPE_WORKGROUP)
#define LST(p, x) __hip_atomic_store(p, x, __ATOMIC_RELAXED, __HIP_MEMORY_SCOPE_WORKGROUP)
#define GLD(p) __hip_atomic_load(p, __ATOMIC_RELAXED, __HIP_MEMORY_SCOPE_AGENT)
#define GST(p, x) __hip_atomic_store(p, x, __ATOMIC_RELAXED, __HIP_MEMORY_SCOPE_AGENT)
#define NOUNROLL _Pragma("clang loop unroll(disable)")
#define FENCE_SCHED() __builtin_amdgcn_sched_barrier(0)
#define PIN(x) asm volatile("" : "+v"(x))
#define BARRIER()                                       \
  do {                                                  \
    asm volatile("s_waitcnt lgkmcnt(0)" ::: "memory");  \
    __builtin_amdgcn_s_barrier();                       \
    FENCE_SCHED();                                      \
    asm volatile("" ::: "memory");                      \
  } while (0)

constexpr int BH = 32, SK = 16384, NMEM = 8, NWAVE = 4;
constexpr int NTHREADS = (NWAVE + 1) * 64;
constexpr int NCH = 32;
constexpr unsigned HEAD_BYTES = SK * 512, KV_BYTES = BH * HEAD_BYTES;
constexpr int SPIN = 1 << 19;
constexpr unsigned CODE_BYTES = 7044;

constexpr int RING = 6;
constexpr int LEAD = 35 * 16;
constexpr int CP0 = 1, CPS = 12, NRD = 5;
constexpr int RDD = 2;
constexpr int MARGIN0 = 14 * 16, MARGIN = 8 * 16, SLOWX = 20 * 16, NOMINAL = 1704, SETTLE = 10 * 16;

constexpr int TST = 1088, DST = 272, DPL = 16;
constexpr int WS_E = 0, WS_T = 64, WS_D = WS_T + 8 * TST, WS_G = (WS_D + 256 / DPL * DST + 31) / 32 * 32;
__device__ __forceinline__ unsigned dpos(unsigned b) { return WS_D + b / DPL * DST + b % DPL; }
constexpr int R4 = 33, T4 = 16 * R4, TILES = 22 * R4;
constexpr int L_O = 0, L_K = 17 * R4, L_W = L_K + NWAVE * TILES, L_T = L_W + NWAVE * NCH * 4, L_C = L_T + 64, L_END = L_C + 6;

__device__ __forceinline__ float asf(unsigned x) { return __builtin_bit_cast(float, x); }
__device__ __forceinline__ unsigned asu(float x) { return __builtin_bit_cast(unsigned, x); }
__device__ __forceinline__ unsigned hi32(u64 x) { return (unsigned)(x >> 32); }
__device__ __forceinline__ unsigned clock10ns() { return (unsigned)__builtin_amdgcn_s_memrealtime(); }
__device__ __forceinline__ float rbf(float x, float zero) {
  return __builtin_bit_cast(float, __builtin_convertvector(f32x2{zero, x}, bf16x2));
}
template <int CTRL> __device__ __forceinline__ int dppi(int x) { return __builtin_amdgcn_mov_dpp(x, CTRL, 0xf, 0xf, true); }
template <int CTRL> __device__ __forceinline__ float dpp(float x) { return asf(dppi<CTRL>(asu(x))); }
constexpr int XOR1 = 0xb1, XOR2 = 0x4e, XOR7 = 0x141, ROR8 = 0x128;
__device__ __forceinline__ f32x4 mfma(u32x4 a, u32x4 b, f32x4 c) {
  return __builtin_amdgcn_mfma_f32_16x16x32_bf16(__builtin_bit_cast(bf16x8, a), __builtin_bit_cast(bf16x8, b), c, 0, 0, 0);
}
__device__ __forceinline__ f32x4 colsum(float x) {
  return __builtin_amdgcn_mfma_f32_16x16x4f32(1.0f, x, f32x4{0.f, 0.f, 0.f, 0.f}, 0, 0, 0);
}
__device__ __forceinline__ float fold32(float a, float b) {
  const auto s = __builtin_amdgcn_permlane32_swap(asu(a), asu(b), false, false);
  return asf(s[0]) + asf(s[1]);
}
__device__ __forceinline__ rsrc_t mkrs(const void* p, unsigned bytes) {
  return __builtin_amdgcn_make_buffer_rsrc((void*)p, 0, bytes, 0x00020000);
}
__device__ __forceinline__ float phi(float dot, float kr, float z0) {
  const float x4 = rbf(fmaf(kr, -0.125f, rbf(dot, z0)), z0);
  return rbf(fmaf(rbf(__builtin_amdgcn_exp2f(x4 * (0.25f * 1.44269504088896341f)), z0), 0.25f, 1e-6f), z0);
}

__global__ void __launch_bounds__(NTHREADS, 2)
rfa_decode(const unsigned char* __restrict__ q, const unsigned char* __restrict__ k, const unsigned char* __restrict__ v,
           const unsigned char* __restrict__ omega, __hip_bfloat16* __restrict__ out, u64* __restrict__ ws_) {
  __shared__ u32x4 l4[L_END];
  float* lds = (float*)l4;
  FENCE_SCHED();
  const int tid = threadIdx.x < 64 ? threadIdx.x + 256 : threadIdx.x - 64, lane = tid & 63;
  const unsigned pf = __builtin_amdgcn_raw_buffer_load_b32(
      mkrs((const void*)(__builtin_amdgcn_s_getpc() & ~127ull), CODE_BYTES), threadIdx.x * 128, 0, 0);
  const int wave = __builtin_amdgcn_readfirstlane(tid >> 6);
  const unsigned bid = blockIdx.x, bh = bid / NMEM, mem = bid % NMEM;
  const int xj = (wave & 1) * 6;
  int vo[4], voq = (lane & 31) * 16;
#pragma unroll
  for (int i = 0; i < 4; ++i) vo[i] = lane * 16 + ((i ^ xj) & 3) * 1024;
  unsigned so0 = bh * HEAD_BYTES + mem * 32768 + wave * 4096 + (xj >> 2) * 16384, so1 = so0 ^ 16384, soq = bh * 512;
  unsigned kbytes = wave == NWAVE ? 0 : KV_BYTES;
  float z0 = 0.f;
  asm volatile("" : "+v"(vo[0]), "+v"(vo[1]), "+v"(vo[2]), "+v"(vo[3]), "+v"(voq), "+s"(so0), "+s"(so1), "+s"(soq), "+s"(kbytes), "+s"(z0));
  FENCE_SCHED();
  asm volatile("" ::"s"(q), "s"(k), "s"(v), "s"(omega), "s"(out), "s"(ws_));
  gu64* ws = (gu64*)ws_;
  lu32* lc = (lu32*)(l4 + L_C);
  lu32* lt = (lu32*)(l4 + L_T);

  const int g = lane >> 4, c = lane & 15;
  const f32x4 z = {0.f, 0.f, 0.f, 0.f};

  u32x4 r[8], om[8], oq;
  u64 e0, pre0, pre1;
  if (wave == NWAVE) {
    oq = __builtin_amdgcn_raw_buffer_load_b128(mkrs(q, BH * 512), voq, soq, 0);
#pragma unroll
    for (int i = 0; i < 8; ++i) om[i] = __builtin_amdgcn_raw_buffer_load_b128(mkrs(omega, 8192), vo[i & 3], (i >> 2) * 4096, 0);
  }
#define LOAD(rs, n, j) __builtin_amdgcn_raw_buffer_load_b128(rs, vo[(j) & 3], (n) * 262144 + ((j) < 4 ? so0 : so1), 2)
  const rsrc_t rk = mkrs(k, kbytes);
#pragma unroll
  for (int j = 0; j < RING; ++j) { r[j] = LOAD(rk, 0, j); FENCE_SCHED(); }
  if (wave == NWAVE) {
    e0 = GLD(ws + WS_E);
    pre0 = GLD(ws + WS_G + (bh * NMEM + (lane >> 1 & 7)) * 256 + mem * 32 + (lane & 1) * 16);
    pre1 = GLD(ws + (bid >= 8 ? WS_E : lane < 32 ? dpos(lane * 8) : WS_T + (lane & 7) * TST));
  }
  l4[L_T + min(tid, 64)] = u32x4{0, 0, 0, 0};
  BARRIER();

  if (wave == NWAVE) {
    const unsigned now0 = clock10ns();
#pragma unroll
    for (int i = 0; i < 8; ++i) l4[L_O + (2 * i + (lane >> 5)) * R4 + (lane & 31)] = om[i];
    l4[L_O + 16 * R4 + (lane & 31)] = oq;
    {
      f32x4 dq = z, gq = z;
#pragma unroll
      for (int s = 0; s < 8; ++s) {
        const u32x4 qf = l4[L_O + 16 * R4 + s * 4 + g];
        dq = mfma(l4[L_O + c * R4 + s * 4 + g], qf, dq);
        gq = mfma(qf, qf, gq);
      }
      const float kq = rbf(gq[0], z0);
      l4[L_C + 2 + g] = u32x4{asu(phi(dq[0], kq, z0)), asu(phi(dq[1], kq, z0)), asu(phi(dq[2], kq, z0)), asu(phi(dq[3], kq, z0))};
    }
    asm volatile("" ::: "memory");
    LST(lc + 3, 1u);
    FENCE_SCHED();
    asm volatile("" ::"v"(pre0), "v"(pre1), "v"(pf));
    const unsigned tag = hi32(e0 * 0x9E3779B97F4A7C15ull + 0x632BE59BD9B4E019ull);
    const u64 th = (u64)tag << 32;
    LST(lc + 2, tag);

    int mprev = 0, cprev = -1, sprev = 0, nrd = NRD;
    bool den_out = false;
    asm volatile("" : "+s"(nrd));
    NOUNROLL for (int rd = 0; rd < nrd; ++rd) {
      const int cp = CP0 + rd * CPS;
      unsigned t;
      NOUNROLL do { __builtin_amdgcn_s_sleep(8); t = LLD(lt + cp * 4 + (lane & 3)); } while (!__all(t != 0));
      asm volatile("" ::: "memory");
      if (cp > NCH && !den_out) {
        const f32x4 d4 = __builtin_bit_cast(f32x4, l4[L_C + 1]);
        if (lane == 0) GST(ws + dpos(bid), th | asu((d4[0] + d4[1]) + (d4[2] + d4[3])));
        den_out = true;
      }
      gu64* gt = ws + WS_T + rd * TST;
      if ((bh & 15) == 8) {
        t -= now0;
        t += dppi<XOR1>(t);
        t += dppi<XOR2>(t);
        if (lane == 0) GST(gt + (bh >> 4) * NMEM + mem, th | ((t >> 2) + now0));
      }
      if (rd == 0) {
        unsigned touch = 0;
        const unsigned th0 = bh * HEAD_BYTES + mem * 8192;
#pragma unroll
        for (int i = 0; i < 4; ++i)
          touch ^= *(cu32*)(v + th0 + i * 2101504) ^ *(cu32*)(k + th0 + i * 2101504 + 1048576);
        asm volatile("" ::"s"(touch));
      }
      u64 x;
      bool got = false;
      NOUNROLL for (int cw = cp + RDD; !got && cw < cp + RDD + 4; ++cw) {
        NOUNROLL do { __builtin_amdgcn_s_sleep(8); } while (LLD(lt + cw * 4) == 0);
        x = GLD(gt + (lane & 15));
        got = __all(hi32(x) == tag);
      }
      if (got) {
        int d = (int)((unsigned)x - now0);
        d += dppi<ROR8>(d);
        int y = max(d, dppi<XOR1>(d));
        y = max(y, dppi<XOR2>(y));
        y = max(y, dppi<XOR7>(y));
        d += dppi<XOR1>(d);
        d += dppi<XOR2>(d);
        d += dppi<XOR7>(d);
        const int mean = __builtin_amdgcn_readfirstlane(d), slowest = __builtin_amdgcn_readfirstlane(y) * 8;
        const int sm = cprev < 0 ? NOMINAL + NOMINAL / 64 : (int)((float)(mean - mprev) * 0.995f / (float)(cp - cprev));
        const int sl = cprev < 0 ? NOMINAL : max(NOMINAL, min(sm, sprev));
        LST((lu64*)lc, (u64)(unsigned)sl << 32 | (now0 * 16 + (unsigned)(max(mean + (cprev < 0 ? MARGIN0 : MARGIN), slowest - SLOWX) - sl * cp)));
        mprev = mean + (cprev < 0 ? SETTLE : 0); cprev = cp; sprev = sm;
      }
    }

    float n, dn;
    bool ok;
    NOUNROLL for (int spin = 0; spin < SPIN; ++spin) {
      const u64 xd = GLD(ws + dpos(bh * NMEM + (lane & 7)));
      dn = asf((unsigned)xd);
      ok = __all(hi32(xd) == tag);
      if (ok) break;
      __builtin_amdgcn_s_sleep(8);
    }
    dn += dpp<XOR1>(dn);
    dn += dpp<XOR2>(dn);
    dn += dpp<XOR7>(dn);
    dn = ok ? 1.0f / rbf(rbf(dn, z0) + 1e-6f, z0) : __builtin_nanf("");
    if (bid == 0) {
      NOUNROLL for (int spin = 0; spin < SPIN; ++spin) {
        gu64* gd = ws + dpos(lane);
        constexpr int S = 64 / DPL * DST;
        const u64 x0 = GLD(gd), x1 = GLD(gd + S), x2 = GLD(gd + 2 * S), x3 = GLD(gd + 3 * S);
        if (__all(hi32(x0) == tag && hi32(x1) == tag && hi32(x2) == tag && hi32(x3) == tag)) break;
        __builtin_amdgcn_s_sleep(8);
      }
      if (lane == 0) GST(ws + WS_E, e0 + 1);
    }

    gu64* gm = ws + WS_G + (bh * NMEM + (lane >> 5) * 4 + 2) * 256 + mem * 32 + (lane & 31);
    gbf16* po = (gbf16*)out + bid * 32 + (lane & 31);
    asm volatile("" : "+v"(gm), "+v"(dn), "+v"(po));
    BARRIER();
    NOUNROLL for (int spin = 0; spin < SPIN; ++spin) {
      const u64 x0 = GLD(gm - 512), x1 = GLD(gm - 256), x2 = GLD(gm), x3 = GLD(gm + 256);
      n = (asf((unsigned)x0) + asf((unsigned)x1)) + (asf((unsigned)x2) + asf((unsigned)x3));
      ok = __builtin_amdgcn_ballot_w64(((hi32(x0) ^ tag) | (hi32(x1) ^ tag) | (hi32(x2) ^ tag) | (hi32(x3) ^ tag)) != 0) == 0;
      if (ok) break;
    }
    n = fold32(n, n);
    if (!ok) dn = __builtin_nanf("");
    *po = (__bf16)(rbf(n, z0) * dn);
    return;
  }

  int lag = -1;
  unsigned now;
  u64 ln;
#define PACE_READ() now = clock10ns(), ln = LLD((lu64*)lc)
#define PACE_EVAL(n)                  \
  LST(lt + (n) * 4 + wave, now | 1);  \
  lag = __builtin_amdgcn_readfirstlane(hi32(ln) == 0 ? -1 : (int)(now * 16 - (unsigned)ln - hi32(ln) * (n)))
#define ARRIVE(j, rc, nc, rn, nn)                                                                       \
  asm volatile("" : "+v"(r[j])::"memory");                                                              \
  asm volatile("s_cmp_ge_i32 %0, 0\n\ts_cbranch_scc1 1f\n\ts_waitcnt vmcnt(%1)\n\t"                     \
               "s_cmp_gt_i32 %0, %2\n\ts_cbranch_scc1 1f\n\ts_waitcnt vmcnt(%3)\n1:"                    \
               ::"s"(lag), "i"(RING - 2), "i"(-LEAD), "i"(RING - 3) : "scc", "memory");                 \
  if ((j) + RING < 8) r[((j) + RING) & 7] = LOAD(rc, nc, ((j) + RING) & 7);                             \
  else r[((j) + RING) & 7] = LOAD(rn, nn, ((j) + RING) & 7);                                            \
  FENCE_SCHED()

  const bool odd = c & 1, hi2 = c & 2, diag = (c >> 2) == g;
  float den = 0.f, kr, wp;
  f32x4 dot, gr, s4;
  float* wl = lds + (L_W + wave * NCH * 4) * 4 + (c & 1) * 8 + (c >> 1);
  const int tw0 = L_K + wave * TILES + (lane >> 5) * R4 + (lane & 31), tr0 = L_K + wave * TILES + c * R4 + g;
  int tw = tw0 + T4, tr = tr0;
  u32x4 fa, fb;
#define FRAGS(s) fa = l4[tr + (s) * 4], fb = l4[tr + (s) * 4 + 4]
#define CHAIN(e)                                         \
  {                                                      \
    const float ph = phi(dot[e], kr, z0);                \
    wp = (e) == 0 ? ph * phq[0] : fmaf(ph, phq[e], wp);  \
  }
#pragma unroll
  for (int j = 0; j < 8; ++j) {
    asm volatile("s_waitcnt vmcnt(%1)" : "+v"(r[j]) : "i"(RING - 2) : "memory");
    r[(j + RING) & 7] = LOAD(rk, (j + RING) >> 3, (j + RING) & 7);
    FENCE_SCHED();
    l4[tw0 + j * 2 * R4] = r[j];
    FENCE_SCHED();
  }
  NOUNROLL while (LLD(lc + 3) == 0) {}
  asm volatile("" ::"v"(pf) : "memory");
#pragma unroll
  for (int s = 0; s < 8; ++s) om[s] = l4[L_O + c * R4 + s * 4 + g];
  const f32x4 phq = __builtin_bit_cast(f32x4, l4[L_C + 2 + g]);
  FRAGS(0);
  int nch = NCH;
  asm volatile("" : "+s"(nch));
  int n = 1;
  NOUNROLL do {
    const rsrc_t rc = mkrs(n >= nch ? v : k, KV_BYTES), rn = mkrs(n + 1 >= nch ? v : k, KV_BYTES);
    const int n0 = n & (NCH - 1), n1 = (n + 1) & (NCH - 1);
#pragma unroll
    for (int j = 0; j < 8; ++j) {
      ARRIVE(j, rc, n0, rn, n1);
      l4[(j >= 3 ? tw0 : tw) + j * 2 * R4] = r[j];
      if (j < 4) {
        dot = mfma(om[2 * j], fa, j == 0 ? z : dot);
        gr = mfma(fa, fa, j == 0 ? z : gr);
        dot = mfma(om[2 * j + 1], fb, dot);
        gr = mfma(fb, fb, gr);
        PIN(dot); PIN(gr);
      }
      if (j < 3) FRAGS(2 * j + 2);
      if (j == 0) PACE_READ();
      if (j == 1) { PACE_EVAL(n); }
      if (j == 4) {
        const float g0 = gr[0], g1 = gr[1], g2 = gr[2], g3 = gr[3];
        const float ge = hi2 ? g2 : g0, go = hi2 ? g3 : g1, gd = odd ? go : ge;
        s4 = colsum(diag ? gd : 0.f);
        PIN(s4);
      }
      if (j == 5) { kr = rbf(s4[0], z0); CHAIN(0) CHAIN(1) PIN(wp); }
      if (j == 6) { CHAIN(2) CHAIN(3) s4 = colsum(wp); PIN(s4); }
      if (j == 7) {
        den += s4[0];
        wl[(n - 1) * 16] = s4[0];
        PIN(den);
        tr = tr0 + (n & 1) * (c >= 6 ? 0 : T4);
        tw = tw0 + (n1 & 1) * T4;
        FRAGS(0);
      }
      FENCE_SCHED();
    }
  } while (++n <= nch);
  den += dpp<XOR1>(den);
  den += dpp<XOR2>(den);
  den += dpp<XOR7>(den);
  den += dpp<ROR8>(den);
  LST(lc + 4 + wave, asu(den));

  float acc[8];
#pragma unroll
  for (int e = 0; e < 8; ++e) acc[e] = 0.f;
#define FMA8(w, x)                                                   \
  _Pragma("unroll") for (int e = 0; e < 4; ++e) {                    \
    const unsigned y = x[e];                                         \
    acc[2 * e] = fmaf(w, asf(y << 16), acc[2 * e]);                  \
    acc[2 * e + 1] = fmaf(w, asf(y & 0xffff0000u), acc[2 * e + 1]);  \
  }                                                                  \
  asm volatile("" : "+v"(acc[0]), "+v"(acc[1]), "+v"(acc[2]), "+v"(acc[3]), "+v"(acc[4]), "+v"(acc[5]), "+v"(acc[6]), "+v"(acc[7]))
  const rsrc_t rv = mkrs(v, KV_BYTES);
  const int wr = L_W + wave * NCH * 4 + (lane >> 5) * 2;
  f32x4 wa = __builtin_bit_cast(f32x4, l4[wr + 4]), wb;
  gu64* gp = ws + WS_G + bid * 256 + tid;
  int lp = L_K + wave * TILES + (lane & 31) * 2 + (lane >> 5);
  asm volatile("" : "+v"(gp), "+v"(lp));
  n = 1;
  NOUNROLL do {
    const rsrc_t rn = mkrs(v, n + 1 < nch ? KV_BYTES : 0);
    float w0;
#pragma unroll
    for (int j = 0; j < 8; ++j) {
      ARRIVE(j, rv, n, rn, n + 1);
      if (j == 0) { PACE_READ(); wb = __builtin_bit_cast(f32x4, l4[wr + n * 4 + 1]); }
      if (j == 1) { PACE_EVAL(n + NCH); lag = n + 1 < nch ? lag : 0; }
      if (j == 2) { fa = l4[tw0 + ((n - 1) & 7) * 2 * R4]; w0 = lds[wr * 4 + ((n - 1) & 7)]; }
      const float w = j < 4 ? wa[j & 3] : wb[j & 3];
      FMA8(w, r[j]);
      if (j == 3 && n <= 8) { FMA8(w0, fa); }
      if (j == 4) wa = __builtin_bit_cast(f32x4, l4[wr + min(n + 1, NCH - 1) * 4]);
      FENCE_SCHED();
    }
  } while (++n < nch);

  l4[lp] = u32x4{asu(fold32(acc[0], acc[4])), asu(fold32(acc[1], acc[5])), asu(fold32(acc[2], acc[6])), asu(fold32(acc[3], acc[7]))};
  BARRIER();
  const unsigned tag = LLD(lc + 2);
  const float* pw = lds + L_K * 4 + tid;
  const float num = (pw[0] + pw[TILES * 4]) + (pw[TILES * 8] + pw[TILES * 12]);
  GST(gp, (u64)tag << 32 | asu(num));
}

extern "C" void kernel_launch(void* const* d_in, const int* in_sizes, int n_in, void* d_out, int out_size, void* d_ws,
                              size_t ws_size, hipStream_t stream) {
  rfa_decode<<<BH * NMEM, NTHREADS, 0, stream>>>((const unsigned char*)d_in[0], (const unsigned char*)d_in[1],
                                                 (const unsigned char*)d_in[2], (const unsigned char*)d_in[3],
                                                 (__hip_bfloat16*)d_out, (u64*)d_ws);
}
```
